```python
import torch

def _softmax(x, axis=-1):
    return torch.softmax(x, dim=axis)
def _gelu(x):
    return torch.nn.functional.gelu(x, approximate="tanh")
def _silu(x):
    return torch.nn.functional.silu(x)
def _sigmoid(x):
    return torch.sigmoid(x)
def _layernorm(x, axis=-1, eps=1e-5):
    m = x.mean(dim=axis, keepdim=True)
    v = x.var(dim=axis, keepdim=True, unbiased=False)
    return (x - m) / torch.sqrt(v + eps)
def _scatter_add(tgt, idx, src, axis=0):
    return tgt.clone().index_add_(axis, idx, src)
def _segment_sum(src, seg, n):
    out = torch.zeros((n,) + src.shape[1:], dtype=src.dtype)
    return out.index_add_(0, seg, src)
def _pad(x, pw):
    flat = tuple(v for pair in reversed(pw) for v in pair)
    return torch.nn.functional.pad(x, flat)

kernel_name = 'standalone__dense_activation_gelu__prompt'

def setup_inputs():

    _w = (lambda a: (lambda t: t.to(torch.bfloat16) if t.is_floating_point() else t)(torch.as_tensor(a)))
    B, S, D, F = 1, 2048, 1024, 3584
    return {
        "x": _w((lambda *s: torch.randn(*s, dtype=torch.bfloat16) * 0.05)(B, S, D)),
        "w1": _w((lambda *s: torch.randn(*s, dtype=torch.bfloat16) * 3.2)(D, F) * (D ** -0.5)),
        "b1": _w((lambda *s: torch.randn(*s, dtype=torch.bfloat16) * 3.2)(F) * 0.01),
        "w2": _w((lambda *s: torch.randn(*s, dtype=torch.bfloat16) * 0.05)(F, D) * (F ** -0.5)),
        "b2": _w((lambda *s: torch.randn(*s, dtype=torch.bfloat16) * 0.1)(D) * 0.01),
    }

def reference(x, w1, b1, w2, b2):
    h = torch.einsum("bsd,df->bsf", x, w1) + b1
    h = _gelu(h)
    return torch.einsum("bsf,fd->bsd", h, w2) + b2
```

```cpp
#include <hip/hip_runtime.h>
#include <hip/hip_bf16.h>
#include <type_traits>

#define LAS __attribute__((address_space(3)))
#define GAS __attribute__((address_space(1)))
typedef short bf16x8 __attribute__((ext_vector_type(8)));
typedef short bf16x4 __attribute__((ext_vector_type(4)));
typedef float f32x4 __attribute__((ext_vector_type(4)));
typedef float f32x2 __attribute__((ext_vector_type(2)));
typedef unsigned u32x4 __attribute__((ext_vector_type(4)));
typedef unsigned u32x2 __attribute__((ext_vector_type(2)));
typedef __bf16 bf2 __attribute__((ext_vector_type(2)));
typedef unsigned long long u64;
typedef __amdgpu_buffer_rsrc_t rsrc_t;

constexpr unsigned WS_H = 0;
constexpr unsigned WS_X = 16u << 20;
constexpr unsigned WS_F = 32u << 20;

#define PRIO_C 1
#define PRIO_L 0
#define ROT2 -1
#define COOP 4
#define SPF 1
#define EPI0 6
#define CX 4
#define XD 2
#define XL 5
#define HD 5
#define PW 32
#define AJ 3

#define HSP 8
#define ABL 0
#define PREP 3800
#define OAUX 16
#define MAP 1
#define ROTT 1
#define TLBW 0
#define TPOS 12
#define NCS 0
#define SLW 4
#define SLD 0
#define SP0 0
#define LOC 1
#define DEF 1
#define XF 3
#define SP2 4
#define WARM 1
#define DI __device__ __forceinline__
#define WAITV(n) asm volatile("s_waitcnt vmcnt(%0)" ::"n"(n) : "memory")
#define BARRIER() asm volatile("s_barrier" ::: "memory")
#define LGKM0_BARRIER() asm volatile("s_waitcnt lgkmcnt(0)\n\ts_barrier" ::: "memory")
#define FENCE() __builtin_amdgcn_sched_barrier(0)
#define VADD(x, d) asm volatile("v_add_u32 %0, %1, %0" : "+v"(x) : "s"(d))

#define HOP(i, next) asm volatile(".Lhop%c0:\n\ts_cbranch_execz .Lhop%c1" :: "n"(i), "n"(next))
#define STAMP(i)
#define RSTAMP(i)
#define STAMP_DECL
#define STAMP_OUT()
#define TR_DECL
#define TR(c)
#define TR_OUT()

DI unsigned cvtpk(float lo, float hi) { f32x2 v = {lo, hi}; bf2 b = __builtin_convertvector(v, bf2); return __builtin_bit_cast(unsigned, b); }
DI bf16x8 lds128(unsigned a) { return *(LAS bf16x8*)(size_t)a; }
DI bf16x4 ldstr(unsigned a) { return __builtin_amdgcn_ds_read_tr16_b64_v4i16((LAS bf16x4*)(size_t)a); }
DI f32x2 bfpair(unsigned b) { return (f32x2){__builtin_bit_cast(float, b << 16), __builtin_bit_cast(float, b & 0xffff0000u)}; }
DI f32x4 bf4(u32x2 b) { const f32x2 l = bfpair(b.x), h = bfpair(b.y); return (f32x4){l.x, l.y, h.x, h.y}; }

#define DMA4(l, base, v0, v1, v2, v3) asm volatile("s_mov_b32 m0, %0\n\ts_nop 0\n\tglobal_load_lds_dwordx4 %2, %1\n\tglobal_load_lds_dwordx4 %3, %1 offset:1024" \
    "\n\tglobal_load_lds_dwordx4 %4, %1 offset:2048\n\tglobal_load_lds_dwordx4 %5, %1 offset:3072" :: "s"(l), "s"(base), "v"(v0), "v"(v1), "v"(v2), "v"(v3) : "memory")
#define DMA3(l, base, v0, v1, v2) asm volatile("s_mov_b32 m0, %0\n\ts_nop 0\n\tglobal_load_lds_dwordx4 %2, %1\n\tglobal_load_lds_dwordx4 %3, %1 offset:1024" \
    "\n\tglobal_load_lds_dwordx4 %4, %1 offset:2048" :: "s"(l), "s"(base), "v"(v0), "v"(v1), "v"(v2) : "memory")
#define DMA1(l, base, v0) asm volatile("s_mov_b32 m0, %0\n\ts_nop 0\n\tglobal_load_lds_dwordx4 %2, %1" :: "s"(l), "s"(base), "v"(v0) : "memory")
#define TOUCH(l, base, voff) asm volatile("s_mov_b32 m0, %0\n\ts_nop 0\n\tglobal_load_lds_dword %2, %1" :: "s"(l), "s"(base), "v"(voff) : "memory")
#define TOUCHS(l, base, voff) asm volatile("s_mov_b32 m0, %0\n\ts_nop 0\n\tglobal_load_lds_dword %2, %1 sc1" :: "s"(l), "s"(base), "v"(voff) : "memory")
#define BDMA4(l, lo, rs, soff, v0, v1, v2, v3, pol) asm volatile("s_add_u32 m0, %0, %1\n\ts_nop 0\n\tbuffer_load_dwordx4 %4, %2, %3 offen" pol " lds\n\tbuffer_load_dwordx4 %5, %2, %3 offen offset:1024" pol " lds" \
    "\n\tbuffer_load_dwordx4 %6, %2, %3 offen offset:2048" pol " lds\n\tbuffer_load_dwordx4 %7, %2, %3 offen offset:3072" pol " lds" \
    :: "s"(l), "n"(lo), "s"(rs), "s"(soff), "v"(v0), "v"(v1), "v"(v2), "v"(v3) : "scc", "memory")
#define BDMA2(l, lo, rs, soff, v0, pol) asm volatile("s_add_u32 m0, %0, %1\n\ts_nop 0\n\tbuffer_load_dwordx4 %4, %2, %3 offen" pol " lds\n\tbuffer_load_dwordx4 %4, %2, %3 offen offset:1024" pol " lds" \
    :: "s"(l), "n"(lo), "s"(rs), "s"(soff), "v"(v0) : "scc", "memory")
template <int LO, bool SC1> DI void bdma4(unsigned l, rsrc_t rs, unsigned soff, unsigned v0, unsigned v1, unsigned v2, unsigned v3) {
  if constexpr (SC1) BDMA4(l, LO, rs, soff, v0, v1, v2, v3, " sc1"); else BDMA4(l, LO, rs, soff, v0, v1, v2, v3, ""); }
template <int LO> DI void bdma2(unsigned l, rsrc_t rs, unsigned soff, unsigned v0) { BDMA2(l, LO, rs, soff, v0, " sc1"); }
#define SLINE(p, off) asm volatile("s_load_dwordx2 s[100:101], %0, %1" :: "s"(p), "n"((off) + 60) : "s100", "s101", "memory")
#define SWORD(p) asm volatile("s_load_dword s100, %0, 0" :: "s"(p) : "s100", "s101", "memory")
#define SLINES(p, soff) asm volatile("s_load_dwordx2 s[100:101], %0, %1" :: "s"(p), "s"(soff) : "s100", "s101", "memory")

constexpr int G1_SLOT = 45056, G1_NT = 16, G1_W = 16384, G1_SS = 14336;
constexpr int G2_N1 = 28 - CX, G2_NT = 28 + CX, G2_W = 98304;
constexpr int LDS_BYTES = 163840;

#define FCLOB "v236", "v237", "v238", "v239", "v240", "v241", "v242", "v243", "v244", "v245", "v246", "v247", "v248", "v249", "v250", "v251", "v252", "v253", "v254", "v255"
DI void flags_init(unsigned vlo) {
  asm volatile("v_not_b32 v236, %0\n\tv_not_b32 v238, %0\n\tv_not_b32 v240, %0\n\tv_not_b32 v242, %0\n\tv_not_b32 v244, %0\n\tv_not_b32 v246, %0\n\tv_not_b32 v248, %0\n\tv_not_b32 v250, %0\n\tv_not_b32 v252, %0\n\tv_not_b32 v254, %0" :: "v"(vlo) : FCLOB); }
template <int I> DI void flags_fetch(unsigned off, const char* base) {
  asm volatile("global_load_dwordx4 v[%c2:%c3], %0, %1 sc1" :: "v"(off), "s"(base), "n"(236 + 4 * I), "n"(239 + 4 * I) : FCLOB, "memory"); }
template <int I> DI unsigned flags_read(u64 V) { u64 a, b;
  asm volatile("v_cmp_eq_u64 %0, v[%c3:%c4], %2\n\tv_cmp_eq_u64 %1, v[%c5:%c6], %2" : "=&s"(a), "=&s"(b) : "v"(V), "n"(236 + 4 * I), "n"(237 + 4 * I), "n"(238 + 4 * I), "n"(239 + 4 * I) : "memory");
  return (unsigned)(a & b); }
DI void pub_store(int k, const char* p, u64 V) { unsigned o;
  asm volatile("s_mov_b64 exec, 1\n\tv_mov_b32 %0, %3\n\tglobal_store_dwordx2 %0, %1, %2 offset:%4 sc1\n\ts_mov_b64 exec, -1" : "=&v"(o) : "v"(V), "s"(p), "n"(k == 3 ? 2048 : 0), "n"(k == 3 ? 2048 : (k - 1) * 2048) : "memory"); }
DI void hop(int i, int next) { HOP(i, next); }
constexpr int req(int t, int st) {
  if (t < 0) return 0;
  if (t >= 16) return 7;
  const int tl = (ROT2 >= 0 && t + 4 >= ROT2) ? t + 8 : t + 4, il = 2 * tl + 1 > 55 ? 55 : 2 * tl + 1, lo = 7 + 14 * st;
  if (il < lo) return 0;
  const int p = 1 + (il - lo) / (st < 2 ? 2 : 3);
  return p > 7 ? 7 : p;
}
template <int I, int N, class F> DI void static_for(F&& f) { if constexpr (I < N) { f(std::integral_constant<int, I>{}); static_for<I + 1, N>(f); } }

__global__ __launch_bounds__(512, 2) void mlp_kernel(const char* X_, const char* W1_, const char* B1_, const char* W2_, const unsigned short* B2_, char* ws_, char* out_) {
  typedef u64 u64x4 __attribute__((ext_vector_type(4))); typedef u64 u64x2 __attribute__((ext_vector_type(2)));
  u64x4 ka; u64x2 kb; u64 kc;
  asm volatile("s_load_dwordx8 %0, %3, 0\n\ts_load_dwordx4 %1, %3, 32\n\ts_load_dwordx2 %2, %3, 48" : "=&s"(ka), "=&s"(kb), "=&s"(kc) : "s"(__builtin_amdgcn_kernarg_segment_ptr()));
  const char* code;
  asm volatile("s_getpc_b64 %0\n.Lmlp_ref:" : "=s"(code));
  { unsigned t_; asm volatile("v_and_b32 %0, 7, %2\n\tv_and_or_b32 %0, %3, -8, %0\n\tv_lshlrev_b32 %0, 7, %0\n\tv_min_u32 %0, .Lmlp_end2-.Lmlp_ref, %0\n\ts_mov_b32 m0, 0x22000\n\ts_nop 0\n\tglobal_load_lds_dword %0, %1"
                              : "=&v"(t_) : "s"(code), "v"(threadIdx.x), "s"(blockIdx.x) : "memory"); }
  FENCE();
  asm volatile("s_waitcnt lgkmcnt(0)" : "+s"(ka), "+s"(kb), "+s"(kc));
  const char *X = (const char*)ka[0], *W1 = (const char*)ka[1], *B1 = (const char*)ka[2], *W2 = (const char*)ka[3];
  const unsigned short* B2 = (const unsigned short*)kb[0]; char *ws = (char*)kb[1], *out = (char*)kc;
  extern __shared__ __attribute__((aligned(16))) char smem_[];
  const unsigned lb = (unsigned)(size_t)(LAS char*)smem_;
  const int b = blockIdx.x, xcd = b & 7, idx = b >> 3;
  const int tm = MAP ? 8 * (xcd >> 2) + (idx & 7) : 4 * (xcd >> 1) + (idx & 3), kh = xcd & 1, nb = MAP ? 4 * ((xcd >> 1) & 1) + (idx >> 3) : idx >> 2, tn = 8 * kh + nb;
  const int wid = __builtin_amdgcn_readfirstlane(threadIdx.x >> 6), lane = threadIdx.x & 63;
  const int odd = tn & 1, nc = (nb + NCS) & 7;
  STAMP_DECL; RSTAMP(14); STAMP(0);
  unsigned csz;
  asm volatile("s_mov_b32 %0, .Lmlp_end2-.Lmlp_ref" : "=s"(csz));
  const char* Fb = ws + WS_F + tm * 16384;
  const int w = wid & 3, gw = b * 4 + w;
  auto spf = [&](int cw, int cx) {
    { const char* p = W1 + (size_t)cw * 262144 + gw * 256; SLINE(p, 0); SLINE(p, 128); }
    { const char* p = X + ((cx & 1) * 1024 + gw) * 2048 + (cx >> 1) * 128; SLINE(p, 0); }
  };
  constexpr unsigned LT = 139520;
  auto loader = [&]() __attribute__((always_inline)) {
    __builtin_amdgcn_s_setprio(PRIO_L);
    static_assert(COOP >= 1 && COOP <= 4, "");
    {
      const int l = min(lane, COOP * 8 - 1);
      const unsigned o0 = (unsigned)(b * 14 + min(lane & 15, 13) + min(lane >> 4, COOP - 1) * 3584) * 128, o1 = (unsigned)((b * 8 + (l & 7)) * 2048 + (l >> 3) * 128);
      const unsigned o2_ = 8192 + (lane & 1) * 4, o3 = tn * 448 + lane * 4;
      const char* bp = w == 0 ? W1 : w == 1 ? X : w == 2 ? Fb : B1;
      TOUCHS(lb + (w < 2 ? 135168 + w * 256 : w == 2 ? 137216 : 136192), bp, w == 0 ? o0 : w == 1 ? o1 : w == 2 ? o2_ : o3);
      if (w == 3) TOUCH(lb + 136192 + 256, B1, tn * 448 + 256 + min(lane, 47) * 4); }
    unsigned ox[4], ow[6], oh;
#pragma unroll
    for (int j = 0; j < 4; ++j) { const int row = 8 * (4 * w + j) + (lane >> 3); ox[j] = (unsigned)((tm * 128 + row) * 2048 + (((lane & 7) ^ ((row >> 1) & 7)) << 4) - j * 1024); }
    const unsigned mx = lb + w * 4096, mw = lb + G1_W + (w >> 1) * G1_SS + (w & 1) * 6144, mh = lb + G1_W + (w >> 1) * G1_SS + 12288 + (w & 1) * 1024;
    STAMP(1);
    DMA4(mx + G1_SLOT, X, ox[0], ox[1], ox[2], ox[3]);
    FENCE();
#pragma unroll
    for (int j = 0; j < 6; ++j) { const int rg = 2 * w + j / 3, jj = j % 3, r8 = lane >> 3, c = lane & 7, h = ((r8 >> 1) & 1) | ((rg & 1) << 1);
      ow[j] = (unsigned)((8 * rg + r8) * 7168 + tn * 448 + odd * 64 + jj * 128 + (((c >> 1) ^ h) << 5) + ((c & 1) << 4) + 2048 - jj * 1024); }
    { const int r16 = lane >> 2, c = lane & 3; oh = (unsigned)((16 * w + r16) * 7168 + tn * 448 + (odd ? 0 : 384) + (((c >> 1) ^ (r16 >> 3)) << 5) + ((c & 1) << 4) + 2048); }
    auto issue1x = [&](int t, unsigned so) { const char* xa = X + t * 128; DMA4(mx + so, xa, ox[0], ox[1], ox[2], ox[3]); };
    auto issue1a = [&](int t, unsigned so) { const char* wa = W1 + (size_t)t * (64 * 7168) - 2048; DMA3(mw + so, wa, ow[0], ow[1], ow[2]); };
    auto issue1b = [&](int t, unsigned so) { const char* wa = W1 + (size_t)t * (64 * 7168) - 2048; DMA3(mw + so + 3072, wa, ow[3], ow[4], ow[5]); DMA1(mh + so, wa, oh); };
    if (SP0 == 0) { spf(7, 8); spf(8, 9); }
    unsigned ptabe;
    {
      const int u = tm * 4 + (lane & 3), kk = min(lane >> 2, 6);
      ptabe = (unsigned)(((7 * tn + kk) * 32 + (u >> 1)) * 2048 + nc * 256 + (u & 1) * 128); }
    unsigned oe; const char* be;
    { const int cls = b & 3, mem = b >> 2, te = EPI0 + cls;
      if (w < 2) { oe = (unsigned)(te * 3584 + mem * 56 + w * 28 + min(lane, 27)) * 128u; be = W1; } else if (w == 2) { oe = (unsigned)((mem * 32 + (lane & 31)) * 2048 + te * 128); be = X; } else { oe = ptabe; be = W2; } }
    const unsigned fo = (((lane >> 1) & 7) ^ (LOC ? nb & 4 : 0)) * 128 + (lane & 1) * 16, fx = nb * 128 + (lane & 1) * 16;
    asm volatile("" : "+v"(oe));
    code = (const char*)((size_t)code & ~(size_t)127) + min((unsigned)(idx * 4 + w) * 256u, (csz - 256u) & ~127u);
    FENCE();
    WAITV(4); BARRIER();
    issue1a(0, G1_SLOT); issue1b(0, G1_SLOT);
    if (SP0 == 1) { spf(7, 8); spf(8, 9); }
    issue1x(1, 2 * G1_SLOT);
    WAITV(4); BARRIER();
    issue1a(1, 2 * G1_SLOT);
    spf(9, 10);
    issue1b(1, 2 * G1_SLOT);
    spf(10, 11);
    issue1x(2, 0); issue1a(2, 0); issue1b(2, 0);
    if (EPI0 >= 0) { const char* p = X + gw * 2048 + 10 * 128; SLINE(p, 0); }
    unsigned so = G1_SLOT;
#pragma unroll 1
    for (int t = 0; t < 11; ++t) {
      WAITV(11); BARRIER();
      issue1x(t + 3, so); issue1a(t + 3, so); issue1b(t + 3, so);
      if (EPI0 >= 0 && t == (b & 3)) TOUCH(lb + 135168 + w * 256, be, oe);
      if (EPI0 >= 0) spf(17 + t, 21 + t);
      else if (t < 10) { spf(min(11 + 2 * t, 27), 12 + 2 * t); spf(min(12 + 2 * t, 27), 13 + 2 * t); }
      so = so == 2 * G1_SLOT ? 0 : so + G1_SLOT;
    }
    unsigned ptab, stab;
    auto spf2 = [&](int c) {
      if (c < 56) { const unsigned o = __builtin_amdgcn_readlane(ptab, c); asm volatile("s_nop 0"); SLINES(W2, o); }
    };
    auto spf3 = [&](int t) { const unsigned o = __builtin_amdgcn_readlane(stab, t); asm volatile("s_nop 0"); SLINES(W2, o); };
    const rsrc_t rw = __builtin_amdgcn_make_buffer_rsrc((void*)W2, 0, 3584 * 2048, 0x00020000);
    const rsrc_t rh = __builtin_amdgcn_make_buffer_rsrc(ws + WS_H + (size_t)tm * (112 * 8192), 0, 112 * 8192, 0x00020000);
    WAITV(11); BARRIER();
    issue1x(14, 0); issue1a(14, 0); issue1b(14, 0);
    SLINE(code, 0); SLINE(code, 128);
    { const char* fl = Fb + w * 2048 + tn * 128; SLINE(fl, 0); }
    WAITV(11); BARRIER();
    issue1x(15, G1_SLOT); issue1a(15, G1_SLOT); issue1b(15, G1_SLOT);
    asm volatile("s_waitcnt lgkmcnt(0)" ::: "memory");
    const u64 V = *(volatile LAS u64*)(size_t)(lb + 137216) + 1;
    const u32x4 ta = *(volatile LAS u32x4*)(size_t)(lb + LT + w * 2048 + lane * 16), o2 = *(volatile LAS u32x4*)(size_t)(lb + LT + w * 2048 + 1024 + lane * 16);
    const unsigned tab = ta[0], ptab0 = ta[2];
    ptab = ta[1]; stab = ta[3];
    const unsigned ohh = lane * 16 + (w & 1) * 4096, ohk = ohh + kh * 2048;
    asm volatile("" :: "v"(V));
    if (EPI0 < 0 && w == 3) TOUCH(mx + G2_W + 3 * 16384 + 2048, W2, ptabe);
    if (TPOS == 12) TOUCH(mx + G2_W + 3 * 16384 + 2048, W2, ptab0);
    unsigned kw[4];
#pragma unroll
    for (int t = 0; t < 4; ++t) kw[t] = __builtin_amdgcn_readlane(tab, t) << 16;
    WAITV(TPOS == 12 ? 12 : 11); BARRIER();
    bdma4<G2_W, false>(mx, rw, kw[0], o2[0], o2[1], o2[2], o2[3]);
    if (TPOS != 12) bdma4<G2_W + 16384, false>(mx, rw, kw[1], o2[0], o2[1], o2[2], o2[3]);
    asm volatile("s_mov_b64 exec, 0\n\ts_cmp_eq_u32 %0, 0\n\ts_cbranch_scc1 .Lhop0\n\ts_cmp_eq_u32 %0, 1\n\ts_cbranch_scc1 .Lhop%c1\n\ts_cmp_eq_u32 %0, 2\n\ts_cbranch_scc1 .Lhop%c2\n\ts_branch .Lhop%c3\n"
                 ".Lhop999:\n\ts_mov_b64 exec, -1" :: "s"(w), "n"(32 / HSP), "n"(64 / HSP), "n"(96 / HSP) : "scc", "memory");
    WAITV(TPOS == 12 ? 5 : 8); BARRIER();
    if (TPOS != 12) { bdma4<G2_W + 2 * 16384, false>(mx, rw, kw[2], o2[0], o2[1], o2[2], o2[3]); TOUCH(lb + 90112 + w * 256, W2, ptab0); }
    BARRIER();
    const u64 tb15 = __builtin_amdgcn_s_memtime();
    if (TPOS == 12) { bdma4<G2_W + 16384, false>(mx, rw, kw[1], o2[0], o2[1], o2[2], o2[3]); bdma4<G2_W + 2 * 16384, false>(mx, rw, kw[2], o2[0], o2[1], o2[2], o2[3]); }
    WAITV(TPOS == 12 ? 0 : 1);
    flags_init((unsigned)V);
    auto fbase = [&](int I) { return Fb + (I < 3 ? I * 2048 + kh * 1024 : (I - 1) * 2048 + (kh ^ 1) * 1024); };
    auto need = [&](auto I, unsigned m) {
      if (__builtin_expect((m & ~flags_read<I>(V)) != 0, 0)) {
#pragma unroll 1
        for (unsigned spins = 0; spins < (1u << 20); ++spins) {
          flags_fetch<I>(I == 4 ? fx : fo, fbase(I)); WAITV(0);
          if ((m & ~flags_read<I>(V)) == 0) break;
        }
      }
    };
    using I0 = std::integral_constant<int, 0>; using I1 = std::integral_constant<int, 1>; using I2 = std::integral_constant<int, 2>;
    using I3 = std::integral_constant<int, 3>; using I4 = std::integral_constant<int, 4>;
    if (w == 0) {
      while (__builtin_amdgcn_s_memtime() - tb15 < PREP) __builtin_amdgcn_s_sleep(1);
      flags_fetch<0>(fo, fbase(0));
    }
#pragma unroll
    for (int t = 4; t < (PW >= 56 ? 4 + SLW : 0); ++t) spf3(t);
    BARRIER();
    STAMP(2);
    bdma4<G2_W + 3 * 16384, false>(mx, rw, kw[3], o2[0], o2[1], o2[2], o2[3]);
    if (DEF && w == 0) flags_fetch<0>(fo, fbase(0));
    unsigned kn = __builtin_amdgcn_readlane(tab, 4), k4 = 0; int late = 0;
    static_for<0, G2_NT - 1>([&](auto T) {
      constexpr int t = T;
      if constexpr (t + 4 < G2_NT) { if constexpr (DEF && t == 2) WAITV(12); else if constexpr (t < G2_N1 - 3) WAITV(16); else if constexpr (t == G2_N1 - 3) WAITV(14); else WAITV(12); } else WAITV((G2_NT - 2 - t) * 6);
      if constexpr (t == 0) STAMP(3);
      if constexpr (DEF && t == 0) {
        if (w == 0) { *(volatile LAS unsigned*)(size_t)(lb + 65536) = ((0xffffu >> (2 * (7 - req(0, 0)))) & ~flags_read<0>(V)) != 0; asm volatile("s_waitcnt lgkmcnt(0)" ::: "memory"); }
      } else if constexpr (req(t, 0) > req(t - 1, 0)) { if (w == 0) need(I0{}, 0xffffu >> (2 * (7 - req(t, 0)))); }
      if constexpr (req(t, 1) > req(t - 1, 1)) { if (w == 0) need(I1{}, 0xffffu >> (2 * (7 - req(t, 1)))); }
      if constexpr (req(t, 2) > req(t - 1, 2)) { if (w == 0) need(I2{}, 0xffffu >> (2 * (7 - req(t, 2)))); }
      if constexpr (t == 0) STAMP(4);
      if constexpr (t == ((ROT2 >= 0 && ROTT) ? 20 : 24)) { if (w == 0) need(I3{}, 0xffffu); }
      if constexpr (t == G2_N1 + XL) { if (w == 0) need(I4{}, 3u); }
      BARRIER();
      if constexpr (DEF && t == 0) { late = __builtin_amdgcn_readfirstlane(*(volatile LAS unsigned*)(size_t)(lb + 65536)); k4 = kn << 13; }
      if constexpr (t <= (DEF ? 1 : 0)) { if (late == t) {
        if (w >= 2) { const unsigned k3 = kw[3] >> 3; bdma4<3 * 16384, true>(mx, rh, k3, ohh, ohh, ohh, ohh); }
        if (DEF) bdma4<0, true>(mx, rh, k4, ohh, ohh, ohh, ohh); } }
      if constexpr (t + 4 < G2_NT) {
        const unsigned k = kn << 13, k8 = kn << 16;
        if constexpr (DEF && t == 0) {}
        else if constexpr (t + 4 < G2_N1) bdma4<(t & 3) * 16384, true>(mx, rh, k, ohh, ohh, ohh, ohh);
        else bdma2<(t & 3) * 16384>(mx + kh * 2048, rh, k, ohk);
        if constexpr (t + 5 < G2_NT) kn = __builtin_amdgcn_readlane(tab, t + 5);
        if constexpr (t >= SP2) spf2(PW + 2 * (t - SP2));
        bdma4<G2_W + (t & 3) * 16384, false>(mx, rw, k8, o2[0], o2[1], o2[2], o2[3]);
        if constexpr (t >= SP2) spf2(PW + 1 + 2 * (t - SP2));
        if constexpr (SLW > 0 && PW + 2 * (t - SP2) >= 56 + SLD && t + 4 + SLW < G2_NT) spf3(t + 4 + SLW);
      }
      if constexpr (t == 0 || t == 2) { if (w == 0) flags_fetch<0>(fo, fbase(0)); }
      if constexpr ((req(t + 3, 1) > 0 && req(t + 2, 1) == 0) || (req(t, 1) > 0 && req(t - 1, 1) == 0)) { if (w == 0) flags_fetch<1>(fo, fbase(1)); }
      if constexpr ((req(t + 3, 2) > 0 && req(t + 2, 2) == 0) || (req(t, 2) > 0 && req(t - 1, 2) == 0)) { if (w == 0) flags_fetch<2>(fo, fbase(2)); }
      if constexpr (t == 16) { if (w == 0) flags_fetch<3>(fo, fbase(3)); }
      if constexpr (t == G2_N1 + XL - 2 || t == G2_N1 + XL - XF) { if (w == 0) flags_fetch<4>(fx, fbase(4)); }
    });
    STAMP(5);
    if (w == 0 && tn == 0) {
      if (lane == 0) __hip_atomic_store((GAS u64*)(Fb + 8192), V, __ATOMIC_RELAXED, __HIP_MEMORY_SCOPE_AGENT);
    }
    asm volatile("s_waitcnt vmcnt(0) lgkmcnt(0)" ::: "memory");
    asm volatile(".Lmlp_end1:");
    STAMP(6); RSTAMP(15); STAMP_OUT();
  };

  auto mfma_waves = [&]() __attribute__((always_inline)) {
  __builtin_amdgcn_s_setprio(PRIO_C);
  const int wr = wid >> 1, wc = wid & 1, fr = lane & 15, g = lane >> 4, q = fr >> 2, p = lane & 3;
  const rsrc_t rh = __builtin_amdgcn_make_buffer_rsrc(ws + WS_H + (size_t)(tm * 112 + 7 * tn) * 8192, 0, 7 * 8192, 0x00020000);
  TR_DECL;
  u64 V;
  const char* Fw = Fb + tn * 128 + wid * 8 + 2048;
  auto publish = [&](int k) { pub_store(k, Fw, V); };
  const unsigned ho0 = (4 * wr + 2 * wc) * 1024u, ho1 = (4 * wr + 2 - 2 * wc) * 1024u, cb0 = (odd + 3 * wc) * 8192, cb3 = (odd ? 0 : 6) * 8192;
  unsigned ah[2], aw2[4];
  f32x4 acc2[4][4];
  {
    unsigned tab, ptab, ptab0, stab;
    { auto kstep = [&](int t, int hs, int n) {
        const int hh = t >= 28 ? kh ^ 1 : kh; if (t >= 28) t -= CX;
        const int i = min(2 * t + hs, 55);
        int pp, r;
        if (i < 7) { pp = 0; r = i; } else if (i < 35) { const int u = (i - 7) % 14; pp = 1 + (u >> 1); r = 3 * (u & 1) + (i >= 21); } else { pp = 1 + (i - 35) / 3; r = (i - 35) % 3; r = r == 0 ? 2 : r + 4; }
        const int xq = LOC ? n & 4 : 0, bl = pp == 0 ? n : ((pp ^ xq) == n ? xq : pp ^ xq);
        int o = pp == 0 ? r : r + (bl & 1); if (o == 7) o = 0;
        return (unsigned)(56 * hh + 7 * bl + o); };
      int t = lane;
      if (ROT2 >= 0) { constexpr int RE = ROT2 + ((G2_N1 - ROT2) & ~3); if (t >= ROT2 && t < RE && (t & 3) == ((tm + nb) & 3)) t = t + 4 < RE ? t + 4 : t + 4 - (RE - ROT2);
        else if (ROTT && t >= G2_N1 && t < G2_NT && (t & 3) == ((tm + nb) & 3)) t = t + 4 < G2_NT ? t + 4 : t + 4 - 2 * CX; }
      tab = kstep(t, w >> 1, nb);
      {
        const int ns = (nb & 4) | (((3 - (int)((size_t)W2 >> 7)) >> 1) & 3), u = idx * 4 + w;
        stab = (kstep(lane, u >> 6, ns) * 32 + ((u >> 1) & 31)) * 2048 + ns * 256 + (u & 1) * 128 + 60; }
      auto chunk = [&](int c) { int bc, oc;
        if (c < 32) { bc = (c & 15) >> 1; oc = 3 * (c & 1) + (c >> 4); } else { bc = (c - 32) / 3; oc = (c - 32) % 3; oc = oc == 0 ? 2 : oc + 4; }
        if (LOC) bc ^= (gw >> 1) & 4;
        oc += bc & 1; if (oc == 7) oc = 0;
        return (unsigned)(((gw >> 9) * 56 + 7 * bc + oc) * 512 + (gw & 511)) * 128u + 60u; };
      ptab = chunk(min(lane, 55)); ptab0 = chunk(min(lane, PW - 1));
    }
    u32x4 o2;
#pragma unroll
    for (int j = 0; j < 4; ++j) { const int r = 16 * (w & 1) + 4 * j + (lane >> 4), c = lane & 15;
      o2[j] = (unsigned)(r * 2048 + nc * 256 + (((c >> 1) ^ (r & 7)) << 5) + ((c & 1) << 4) - j * 1024); }
    *(LAS u32x4*)(size_t)(lb + LT + w * 2048 + lane * 16) = (u32x4){tab, ptab, ptab0, stab};
    *(LAS u32x4*)(size_t)(lb + LT + w * 2048 + 1024 + lane * 16) = o2;
  }
  {
    unsigned ax[2][2], aw[6];
#pragma unroll
    for (int s = 0; s < 2; ++s)
#pragma unroll
      for (int u = 0; u < 2; ++u) ax[s][u] = lb + G1_SLOT + (64 * wr + 32 * (u ^ wc) + fr) * 128 + (((4 * s + g) ^ (fr >> 1)) << 4);
#pragma unroll
    for (int i = 0; i < 4; ++i) { const int v = 6 * wc + i, j = v >> 2, s4 = v & 3, h = (q >> 1) | ((g & 1) << 1);
      aw[i] = lb + G1_SLOT + G1_W + g * 3072 + j * 1024 + q * 128 + ((s4 ^ h) << 5) + p * 8; }
#pragma unroll
    for (int u = 0; u < 2; ++u) aw[4 + u] = lb + G1_SLOT + G1_W + 12288 + (g >> 1) * 1024 + (8 * (g & 1) + q) * 64 + ((u ^ (g & 1)) << 5) + p * 8;
    f32x4 acc[4][8];
    asm volatile("s_waitcnt vmcnt(0) lgkmcnt(0)\n\ts_barrier" ::: "memory");
    { const u32x2 e = *(volatile LAS u32x2*)(size_t)(lb + 137216);
      V = (((u64)(unsigned)__builtin_amdgcn_readfirstlane(e.y) << 32) | (unsigned)__builtin_amdgcn_readfirstlane(e.x)) + 1; }
#pragma unroll
    for (int i = 0; i < 8; ++i) {
      const int col = (i < 6 ? odd * 32 + 16 * (6 * wc + i) : (odd ? 0 : 192) + 16 * (i - 6)) + 4 * g;
      const f32x4 bv = bf4(*(LAS u32x2*)(size_t)(lb + 136192 + col * 2));
#pragma unroll
      for (int m = 0; m < (i < 6 ? 4 : 2); ++m) acc[m][i] = bv;
    }
    bf16x8 xf[2][4];
    bf16x4 wl[2][8], wh[2][8];
    auto rd1 = [&](int set, int s, int r) {
      if (r < 4) xf[set][r] = lds128(ax[s][r >> 1] + (r & 1) * 2048);
      else { const int i = (r - 4) >> 1, hi = (r - 4) & 1;
        const bf16x4 v = ldstr(i < 6 ? aw[i & 3] + (i >> 2) * 1024 + s * G1_SS + hi * 512 : aw[i - 2] + s * G1_SS + hi * 256);
        if (hi) wh[set][i] = v; else wl[set][i] = v; }
    };
    auto mf = [&](int set, int n) { const int i = n < 24 ? n >> 2 : 6 + ((n - 24) >> 1), m = n < 24 ? ((i & 1) ? 3 - (n & 3) : n & 3) : ((i & 1) ? 1 - (n & 1) : n & 1);
      acc[m][i] = __builtin_amdgcn_mfma_f32_16x16x32_bf16(__builtin_shufflevector(wl[set][i], wh[set][i], 0, 1, 2, 3, 4, 5, 6, 7), xf[set][m], acc[m][i], 0, 0, 0); };
    constexpr int ORD[20] = {0, 4, 5, 1, 2, 3, 6, 7, 8, 9, 10, 11, 12, 13, 14, 15, 16, 17, 18, 19};
#pragma unroll
    for (int i = 0; i < 8; ++i)
#pragma unroll
      for (int m = 0; m < (i < 6 ? 4 : 2); ++m) asm volatile("" : "+v"(acc[m][i]));
    STAMP(1);
    BARRIER();
    STAMP(2);
    __builtin_amdgcn_s_waitcnt(0xC07F);
    FENCE();
#pragma unroll
    for (int r = 0; r < 20; ++r) { rd1(0, 0, ORD[r]); FENCE(); }
    TR(63);
    int ph = 1, delta = G1_SLOT, cnt = G1_NT - 1;
#pragma unroll 1
    do {
#pragma unroll
      for (int n = 0; n < 28; ++n) {
        mf(0, n); FENCE();
        if (n < 20) rd1(1, 1, ORD[n]);
        else if (n < 22) VADD(ax[0][n - 20], delta);
        else VADD(aw[n - 22], delta);
        FENCE();
      }
      LGKM0_BARRIER();
      TR(cnt + 45);
      FENCE();
#pragma unroll
      for (int n = 0; n < 28; ++n) {
        mf(1, n); FENCE();
        if (n < 20) rd1(0, 0, ORD[n]);
        else if (n < 22) VADD(ax[1][n - 20], delta);
        else if (n == 22) asm volatile("s_add_i32 %0, %0, 1\n\ts_cmp_eq_u32 %0, 3\n\ts_cselect_b32 %0, 0, %0" : "+s"(ph) :: "scc");
        else if (n == 24) asm volatile("s_cmp_eq_u32 %1, 2\n\ts_cselect_b32 %0, %2, %3" : "=s"(delta) : "s"(ph), "s"(-2 * G1_SLOT), "s"(G1_SLOT) : "scc");
        else if (n == 25) asm volatile("s_add_i32 %0, %0, -1" : "+s"(cnt) :: "scc");
        FENCE();
      }
    } while (cnt != 0);
#pragma unroll
    for (int n = 0; n < 28; ++n) {
      mf(0, n); FENCE();
      if (n < 20) rd1(1, 1, ORD[n]);
      FENCE();
    }
    LGKM0_BARRIER();
    FENCE();
#pragma unroll
    for (int n = 0; n < 28; ++n) {
      mf(1, n); FENCE();
      if (n >= 12) { acc2[(n - 12) >> 2][n & 3] = (f32x4){0.f, 0.f, 0.f, 0.f}; asm volatile("" : "+v"(acc2[(n - 12) >> 2][n & 3])); }
      else if (n == 0) { ah[0] = lb + (4 * wr + 2 * kh) * 1024 + lane * 16; asm volatile("" : "+v"(ah[0])); }
      else if (n == 1) { ah[1] = lb + (4 * wr + 2 - 2 * kh) * 1024 + lane * 16; asm volatile("" : "+v"(ah[1])); }
      else if (n >= 4 && (n & 1) == 0) { const int i = (n - 4) >> 1;
        aw2[i] = lb + G2_W + (4 * g + q) * 256 + (((4 * wc + i) ^ (4 * (g & 1) + q)) << 5) + p * 8; asm volatile("" : "+v"(aw2[i])); }
      FENCE();
    }
    STAMP(3); RSTAMP(4);
    TR(36);
    const float c1 = -2.302208198f, c2 = -0.1029432397f;
    const unsigned l16 = lane * 16, hv[2] = {ho0 + l16, ho1 + l16}, la[2] = {lb + cb0 + hv[0], lb + cb0 + hv[1]}, la3 = lb + cb3 + hv[0];
    f32x2 z[2][4];
    u32x4 y;
    auto xv = [&](int c, int i) { const f32x4 a = acc[c & 3][2 * (c >> 2) + (i >> 1)]; return (i & 1) ? (f32x2){a[2], a[3]} : (f32x2){a[0], a[1]}; };
    auto plain = [&](int c, int k) {
      const int p = c & 1, q = p ^ 1;
      if (k < 4) { if (c > 0) z[q][k] = z[q][k] * xv(c - 1, k); }
      else if (k < 8) { if (c > 0) y[k - 4] = cvtpk(z[q][k - 4].x, z[q][k - 4].y); }
      else if (k == 8 || k == 11 || k == 14 || k == 16) { const int i = k == 8 ? 0 : k == 11 ? 1 : k == 14 ? 2 : 3; if (c >= 0 && c < 14) z[p][i] = z[p][i] + 1.0f; }
      else if (c < 13) { const int r = k - 9 - (k > 11) - (k > 14) - (k > 16), i = r & 3;
        if (r < 4) z[q][i] = xv(c + 1, i) * xv(c + 1, i); else if (r < 8) z[q][i] = z[q][i] * c2 + c1; else z[q][i] = z[q][i] * xv(c + 1, i); }
    };
    auto trans = [&](int c, int k) { const int p = c & 1, i = (k & 7) >> 1;
      if (c >= 0 && c < 14) { if (k < 8) z[p][i][k & 1] = __builtin_amdgcn_exp2f(z[p][i][k & 1]); else z[p][i][k & 1] = __builtin_amdgcn_rcpf(z[p][i][k & 1]); } };
#pragma unroll
    for (int c = -1; c <= 14; ++c) {
#pragma unroll
      for (int k = 0; k < 8; ++k) {
        if (WARM && (k % HSP) == 0) { constexpr int PP = 8 / HSP; const int h = (c + 1) * PP + k / HSP; hop(h, (h % (4 * PP)) == 4 * PP - 1 ? 999 : h + 1); }
        trans(c, 2 * k); FENCE(); plain(c, 3 * k); FENCE(); plain(c, 3 * k + 1); FENCE();
        trans(c, 2 * k + 1); FENCE(); plain(c, 3 * k + 2); FENCE();
        if (c > 0) { const int d = c - 1, ip = d >> 2, m = d & 3;
          if (k == 2 && !(ABL & 1)) { __builtin_amdgcn_raw_buffer_store_b128(y, rh, hv[m >> 1] + (m & 1) * 1024, ip < 3 ? cb0 + ip * 8192 : cb3, 16); FENCE(); }
          if (k == 4 && (ABL & 2)) asm volatile("" :: "v"(y));
          if (k == 4 && !(ABL & 2)) { *(LAS u32x4*)(size_t)((ip < 3 ? la[m >> 1] + ip * 8192 : la3) + (m & 1) * 1024) = y; FENCE(); }
          if (k == 5 && d == 2 * AJ - 1) {
            WAITV(2 * AJ - 4); publish(0);
            FENCE(); }
          if (k == 5 && d == 13) { WAITV(6); publish(1); FENCE(); }
        }
      }
    }
    TR(43);
  }
  auto& acc = acc2; auto& aw = aw2;
  bf16x8 hf[2][4];
  bf16x4 wl[2][4], wh[2][4];
  auto rd1 = [&](int set, int s, int r) {
    if (r < 4) hf[set][r] = lds128(ah[r >> 1] + (r & 1) * 1024 + s * 8192);
    else { const int i = (r - 4) >> 1, hi = (r - 4) & 1;
      const bf16x4 v = ldstr(aw[i] + s * 8192 + hi * 4096);
      if (hi) wh[set][i] = v; else wl[set][i] = v; }
  };
  auto mf = [&](int set, int n) { const int i = n >> 2, m = (i & 1) ? 3 - (n & 3) : n & 3;
    acc[m][i] = __builtin_amdgcn_mfma_f32_16x16x32_bf16(__builtin_shufflevector(wl[set][i], wh[set][i], 0, 1, 2, 3, 4, 5, 6, 7), hf[set][m], acc[m][i], 0, 0, 0); };
  constexpr int ORD[12] = {0, 4, 5, 1, 2, 3, 6, 7, 8, 9, 10, 11};
  LGKM0_BARRIER();
  STAMP(5);
  __builtin_amdgcn_s_waitcnt(0xC07F);
  FENCE();
#pragma unroll
  for (int r = 0; r < 12; ++r) { rd1(0, 0, ORD[r]); FENCE(); }
  TR(62);
  int delta = 16384, cnt = 0;
  auto p1 = [&](int tend) {
#pragma unroll 1
    do {
#pragma unroll
      for (int n = 0; n < 16; ++n) {
        mf(0, n); FENCE();
        if (n < 12) rd1(1, 1, ORD[n]);
        else if (n < 14) VADD(ah[n - 12], delta);
        else VADD(aw[n - 14], delta);
        FENCE();
      }
#pragma unroll
      for (int n = 0; n < 16; ++n) {
        mf(1, n); FENCE();
        if (n < 2) VADD(aw[2 + n], delta);
        else if (n == 2) asm volatile("s_add_i32 %0, %0, 1" : "+s"(cnt) :: "scc");
        else if (n == 3) { LGKM0_BARRIER(); TR(cnt - 1); }
        else rd1(0, 0, ORD[n - 4]);
        if (n == 8) asm volatile("s_andn2_b32 %0, 3, %1\n\ts_cselect_b32 %0, %3, %2" : "=&s"(delta) : "s"(cnt), "s"(-3 * 16384), "s"(16384) : "scc");
        FENCE();
      }
    } while (cnt != tend);
  };
  p1(HD);
  STAMP(8);
  WAITV(0); publish(2);
  STAMP(11);
  FENCE();
  p1(G2_N1);
  STAMP(6); RSTAMP(13);
  typedef _Float16 h16x2 __attribute__((ext_vector_type(2)));
  const rsrc_t rx = __builtin_amdgcn_make_buffer_rsrc(ws + WS_X, 0, 1024 * 5120, 0x00020000);
  const int so = ((b ^ 1) * 4 + wid) * 5120, ro = (b * 4 + wid) * 5120;
  float xs;
  { float mxv = 0.f;
#pragma unroll
    for (int k = 0; k < 8; ++k) { const f32x4 a = acc[2 + (k >> 2)][k & 3];
      mxv = fmaxf(fmaxf(mxv, fabsf(a[0])), fabsf(a[1])); mxv = fmaxf(fmaxf(mxv, fabsf(a[2])), fabsf(a[3])); }
    const int e = max(__builtin_amdgcn_frexp_expf(fminf(mxv, 3.0e38f)), -100);
    xs = __builtin_amdgcn_ldexpf(1.f, 15 - e);
    __builtin_amdgcn_raw_buffer_store_b32(__builtin_bit_cast(unsigned, __builtin_amdgcn_ldexpf(1.f, e - 15)), rx, lane * 4 + 4096, so, 16); }
  FENCE();
  auto pkh = [&](float lo, float hi) { const f32x2 v = (f32x2){lo, hi} * xs; return __builtin_bit_cast(unsigned, __builtin_convertvector(v, h16x2)); };
  auto xst = [&](int k) { const f32x4 a = acc[2 + (k >> 1)][2 * (k & 1)], c = acc[2 + (k >> 1)][2 * (k & 1) + 1];
    __builtin_amdgcn_raw_buffer_store_b128((u32x4){pkh(a[0], a[1]), pkh(a[2], a[3]), pkh(c[0], c[1]), pkh(c[2], c[3])}, rx, lane * 16 + k * 1024, so, 16); };
  u32x4 pv[4];
  u32x2 braw[4];
  float ps;
  auto xld = [&]() {
#pragma unroll
    for (int k = 0; k < 4; ++k) pv[k] = __builtin_amdgcn_raw_buffer_load_b128(rx, lane * 16 + k * 1024, ro, 16);
    ps = __builtin_bit_cast(float, __builtin_amdgcn_raw_buffer_load_b32(rx, lane * 4 + 4096, ro, 16)); };
  auto unh = [&](unsigned u0, unsigned u1) { const h16x2 p = __builtin_bit_cast(h16x2, u0), q_ = __builtin_bit_cast(h16x2, u1); return (f32x4){(float)p[0], (float)p[1], (float)q_[0], (float)q_[1]}; };
  constexpr int ORT[10] = {0, 4, 5, 1, 6, 7, 8, 9, 10, 11};
  auto mt = [&](int set, int n) { const int i = n >> 1, m = (n ^ i) & 1;
    acc[m][i] = __builtin_amdgcn_mfma_f32_16x16x32_bf16(__builtin_shufflevector(wl[set][i], wh[set][i], 0, 1, 2, 3, 4, 5, 6, 7), hf[set][m], acc[m][i], 0, 0, 0); };
#pragma unroll
  for (int tt = 0; tt < 2 * CX; ++tt) {
    const int dl = ((G2_N1 + tt) & 3) == 3 ? -3 * 16384 : 16384;
#pragma unroll
    for (int n = 0; n < 8; ++n) {
      mt(0, n); FENCE();
      if (n < 5) { rd1(1, 1, ORT[2 * n]); rd1(1, 1, ORT[2 * n + 1]); }
      else if (tt == 0 && n < 7) xst(n - 5);
      FENCE();
    }
    if (tt < 2 * CX - 1) {
      ah[0] += dl; aw[0] += dl; aw[1] += dl; aw[2] += dl; aw[3] += dl;
      if (tt == XD) { WAITV(0); publish(3); }
      LGKM0_BARRIER(); TR(G2_N1 + tt);
      FENCE();
      if (tt == XL) {
#pragma unroll
        for (int i = 0; i < 4; ++i) braw[i] = *(const GAS u32x2*)(B2 + nc * 128 + 64 * wc + 16 * i + 4 * g);
        xld();
        FENCE();
      }
    }
#pragma unroll
    for (int n = 0; n < 8; ++n) {
      mt(1, n); FENCE();
      if (tt < 2 * CX - 1 && n < 5) { rd1(0, 0, ORT[2 * n]); rd1(0, 0, ORT[2 * n + 1]); }
      else if (tt == 0 && n < 7) xst(2 + n - 5);
      FENCE();
    }
  }
  STAMP(7);
  STAMP(12);
  const rsrc_t ro_ = __builtin_amdgcn_make_buffer_rsrc(out + (size_t)tm * (128 * 2048), 0, 128 * 2048, 0x00020000);
  f32x4 bv[4];
#pragma unroll
  for (int i = 0; i < 4; ++i) bv[i] = bf4(braw[i]);
#pragma unroll
  for (int m = 0; m < 2; ++m) {
    const unsigned op = (unsigned)((64 * wr + 16 * (2 * kh + m) + fr) * 2048 + (nc * 128 + 64 * wc) * 2 + (g & 1) * 32 + (g >> 1) * 16);
#pragma unroll
    for (int ip = 0; ip < 2; ++ip) {
      const f32x4 a = unh(pv[2 * m + ip][0], pv[2 * m + ip][1]) * ps + (acc[m][2 * ip] + bv[2 * ip]);
      const f32x4 c = unh(pv[2 * m + ip][2], pv[2 * m + ip][3]) * ps + (acc[m][2 * ip + 1] + bv[2 * ip + 1]);
      const unsigned a0 = cvtpk(a[0], a[1]), a1 = cvtpk(a[2], a[3]), c0 = cvtpk(c[0], c[1]), c1 = cvtpk(c[2], c[3]);
      const auto r0 = __builtin_amdgcn_permlane16_swap(a0, c0, false, false);
      const auto r1 = __builtin_amdgcn_permlane16_swap(a1, c1, false, false);
      const u32x4 o = {r0[0], r1[0], r0[1], r1[1]};
      __builtin_amdgcn_raw_buffer_store_b128(o, ro_, op + ip * 64, 0, OAUX);
    }
  }
  asm volatile(".Lmlp_end2:");
  TR_OUT();
  };
  if (wid < 4) mfma_waves(); else loader();
}

extern "C" void kernel_launch(void* const* d_in, const int* in_sizes, int n_in, void* d_out, int out_size, void* d_ws, size_t ws_size,
                              hipStream_t stream) {
  (void)hipFuncSetAttribute((const void*)mlp_kernel, hipFuncAttributeMaxDynamicSharedMemorySize, LDS_BYTES);
  mlp_kernel<<<256, 512, LDS_BYTES, stream>>>((const char*)d_in[0], (const char*)d_in[1], (const char*)d_in[2], (const char*)d_in[3],
                                              (const unsigned short*)d_in[4], (char*)d_ws, (char*)d_out);
}
```
